```python
import torch

def _softmax(x, axis=-1):
    return torch.softmax(x, dim=axis)
def _gelu(x):
    return torch.nn.functional.gelu(x, approximate="tanh")
def _silu(x):
    return torch.nn.functional.silu(x)
def _sigmoid(x):
    return torch.sigmoid(x)
def _layernorm(x, axis=-1, eps=1e-5):
    m = x.mean(dim=axis, keepdim=True)
    v = x.var(dim=axis, keepdim=True, unbiased=False)
    return (x - m) / torch.sqrt(v + eps)
def _scatter_add(tgt, idx, src, axis=0):
    return tgt.clone().index_add_(axis, idx, src)
def _segment_sum(src, seg, n):
    out = torch.zeros((n,) + src.shape[1:], dtype=src.dtype)
    return out.index_add_(0, seg, src)
def _pad(x, pw):
    flat = tuple(v for pair in reversed(pw) for v in pair)
    return torch.nn.functional.pad(x, flat)

kernel_name = 'standalone__mha_softmax__sampling'

def setup_inputs():

    _w = (lambda a: (lambda t: t.to(torch.bfloat16) if t.is_floating_point() else t)(torch.as_tensor(a)))
    B, H, Sq, Sk, D = 32, 32, 1, 2048, 128
    return {
        "q": _w((lambda *s: torch.randn(*s, dtype=torch.bfloat16) * 0.8)(B, H, Sq, D)),
        "k": _w((lambda *s: torch.randn(*s, dtype=torch.bfloat16) * 0.8)(B, H, Sk, D)),
        "v": _w((lambda *s: torch.randn(*s, dtype=torch.bfloat16) * 0.05)(B, H, Sk, D)),
    }

def reference(q, k, v):
    scale = q.shape[-1] ** -0.5
    scores = torch.einsum("bhqd,bhkd->bhqk", q, k) * scale
    p = _softmax(scores, axis=-1)
    return torch.einsum("bhqk,bhkd->bhqd", p, v)
```

```cpp
#include <hip/hip_runtime.h>
#include <hip/hip_bf16.h>
#include <stdint.h>

typedef __attribute__((ext_vector_type(2))) __bf16 bf16x2_t;
typedef __attribute__((ext_vector_type(4))) unsigned u32x4;
typedef __attribute__((ext_vector_type(4))) float f32x4;

constexpr int D = 128;
constexpr int SK = 2048;
constexpr int WAVES = 4;
constexpr int KEYS_PER_WAVE = SK / WAVES;
constexpr int T = 4;
constexpr int KEYS_PER_STAGE = 4 * T;
constexpr int NSTAGE = KEYS_PER_WAVE / KEYS_PER_STAGE;

__device__ __forceinline__ float dot2(unsigned k, unsigned q, float acc) {
  return __builtin_amdgcn_fdot2_f32_bf16(__builtin_bit_cast(bf16x2_t, k), __builtin_bit_cast(bf16x2_t, q), acc, false);
}
template <int CTRL> __device__ __forceinline__ float dpp(float x) {
  return __builtin_bit_cast(float, __builtin_amdgcn_mov_dpp(__builtin_bit_cast(int, x), CTRL, 0xf, 0xf, true));
}
constexpr int XOR1 = 0xB1, XOR2 = 0x4E, XOR7 = 0x141, XOR8 = 0x128;
__device__ __forceinline__ float row_sum(float x) {
  x += dpp<XOR1>(x); x += dpp<XOR2>(x); x += dpp<XOR7>(x); x += dpp<XOR8>(x);
  return x;
}
__device__ __forceinline__ u32x4 ldnt(const uint16_t* p) { return __builtin_nontemporal_load(reinterpret_cast<const u32x4*>(p)); }

__global__ void __launch_bounds__(WAVES * 64) attn_decode(const uint16_t* __restrict__ q, const uint16_t* __restrict__ k,
                                                          const uint16_t* __restrict__ v, uint16_t* __restrict__ out) {
  __shared__ __attribute__((aligned(16))) float lds[WAVES * 4 * (D + 4)];
  const int head = blockIdx.x;
  const int tid = threadIdx.x, lane = tid & 63, wave = __builtin_amdgcn_readfirstlane(tid >> 6);
  const int r = lane >> 4, c = lane & 15;
  constexpr float C2 = 0.088388347648318440f * 1.4426950408889634f;
  const size_t base = (size_t)head * SK * D + (size_t)wave * KEYS_PER_WAVE * D + (size_t)lane * 8;
  const uint16_t* kp = k + base;
  const uint16_t* vp = v + base;
  u32x4 Kn[T], Vn[T];
#pragma unroll
  for (int t = 0; t < T; ++t) Kn[t] = ldnt(kp + t * 4 * D);
#pragma unroll
  for (int t = 0; t < T; ++t) Vn[t] = ldnt(vp + t * 4 * D);
  const u32x4 qv = *reinterpret_cast<const u32x4*>(q + (size_t)head * D + c * 8);
  float m = -INFINITY, l = 0.f, acc[8];
#pragma unroll
  for (int i = 0; i < 8; ++i) acc[i] = 0.f;
  for (int s = 0; s < NSTAGE; ++s) {
    u32x4 Kc[T], Vc[T];
#pragma unroll
    for (int t = 0; t < T; ++t) { Kc[t] = Kn[t]; Vc[t] = Vn[t]; }
    if (s + 1 < NSTAGE) {
      const int off = (s + 1) * KEYS_PER_STAGE * D;
#pragma unroll
      for (int t = 0; t < T; ++t) Kn[t] = ldnt(kp + off + t * 4 * D);
#pragma unroll
      for (int t = 0; t < T; ++t) Vn[t] = ldnt(vp + off + t * 4 * D);
    }
    float sc[T];
#pragma unroll
    for (int t = 0; t < T; ++t) {
      float d = 0.f;
#pragma unroll
      for (int j = 0; j < 4; ++j) d = dot2(Kc[t][j], qv[j], d);
      sc[t] = row_sum(d) * C2;
    }
    float mn = m;
#pragma unroll
    for (int t = 0; t < T; ++t) mn = fmaxf(mn, sc[t]);
    const float alpha = __builtin_amdgcn_exp2f(m - mn);
    m = mn;
    l *= alpha;
#pragma unroll
    for (int i = 0; i < 8; ++i) acc[i] *= alpha;
#pragma unroll
    for (int t = 0; t < T; ++t) {
      const float p = __builtin_amdgcn_exp2f(sc[t] - mn);
      l += p;
#pragma unroll
      for (int j = 0; j < 4; ++j) {
        acc[2 * j] = fmaf(p, __builtin_bit_cast(float, Vc[t][j] << 16), acc[2 * j]);
        acc[2 * j + 1] = fmaf(p, __builtin_bit_cast(float, Vc[t][j] & 0xffff0000u), acc[2 * j + 1]);
      }
    }
  }
  float* rec = lds + (wave * 4 + r) * (D + 4);
  *reinterpret_cast<f32x4*>(rec + 4 + 8 * c) = f32x4{acc[0], acc[1], acc[2], acc[3]};
  *reinterpret_cast<f32x4*>(rec + 8 + 8 * c) = f32x4{acc[4], acc[5], acc[6], acc[7]};
  if (c == 0) { rec[0] = m; rec[1] = l; }
  __syncthreads();
  if (tid < 64) {
    float M = -INFINITY;
#pragma unroll
    for (int w = 0; w < WAVES * 4; ++w) M = fmaxf(M, lds[w * (D + 4)]);
    float L = 0.f, o0 = 0.f, o1 = 0.f;
#pragma unroll
    for (int w = 0; w < WAVES * 4; ++w) {
      const float* rw = lds + w * (D + 4);
      const float wt = __builtin_amdgcn_exp2f(rw[0] - M);
      L = fmaf(wt, rw[1], L);
      o0 = fmaf(wt, rw[4 + 2 * tid], o0);
      o1 = fmaf(wt, rw[5 + 2 * tid], o1);
    }
    const float inv = 1.f / L;
    __hip_bfloat162 o = __halves2bfloat162(__float2bfloat16(o0 * inv), __float2bfloat16(o1 * inv));
    reinterpret_cast<__hip_bfloat162*>(out + (size_t)head * D)[tid] = o;
  }
}

extern "C" void kernel_launch(void* const* d_in, const int* in_sizes, int n_in, void* d_out, int out_size, void* d_ws,
                              size_t ws_size, hipStream_t stream) {
  const uint16_t* q = (const uint16_t*)d_in[0];
  const uint16_t* k = (const uint16_t*)d_in[1];
  const uint16_t* v = (const uint16_t*)d_in[2];
  const int heads = in_sizes[0] / D;
  attn_decode<<<heads, WAVES * 64, 0, stream>>>(q, k, v, (uint16_t*)d_out);
}
```

```cpp
#define WIN 6
#include <hip/hip_runtime.h>
#include <hip/hip_bf16.h>
#include <stdint.h>

typedef __attribute__((ext_vector_type(2))) __bf16 bf16x2_t;
typedef __attribute__((ext_vector_type(4))) unsigned u32x4;
typedef __attribute__((ext_vector_type(4))) float f32x4;

constexpr int D = 128;
constexpr int SK = 2048;
constexpr int WAVES = 4;
constexpr int HPW = 4;
constexpr int U = WIN;
constexpr int NS = 32;
constexpr int CHUNK_BYTES = 64 * 1024;
constexpr int CHUNKS_PER_HEAD = SK * D * 2 / CHUNK_BYTES;
constexpr int REC = D + 4;
constexpr int LDS_FLOATS = 21 * 1024;

__device__ __forceinline__ float dot2(unsigned k, unsigned q, float acc) {
  return __builtin_amdgcn_fdot2_f32_bf16(__builtin_bit_cast(bf16x2_t, k), __builtin_bit_cast(bf16x2_t, q), acc, false);
}
template <int CTRL> __device__ __forceinline__ float dpp(float x) {
  return __builtin_bit_cast(float, __builtin_amdgcn_mov_dpp(__builtin_bit_cast(int, x), CTRL, 0xf, 0xf, true));
}
constexpr int XOR1 = 0xB1, XOR2 = 0x4E, XOR7 = 0x141, XOR8 = 0x128, BCAST = 0x150;
template <int CTRL> __device__ __forceinline__ float tr_step(float x0, float x1, bool hi) {
  const float keep = hi ? x1 : x0, send = hi ? x0 : x1;
  return keep + dpp<CTRL>(send);
}
template <int T> struct Lane { static constexpr int v = ((T & 1) << 3) | ((T & 2) << 1) | ((T & 4) >> 1) | ((T & 8) >> 3); };

template <int T> __device__ __forceinline__ void pv(const u32x4& vv, float p, float (&acc)[8]) {
  const float pb = dpp<BCAST + Lane<T>::v>(p);
#pragma unroll
  for (int j = 0; j < 4; ++j) {
    acc[2 * j] = fmaf(pb, __builtin_bit_cast(float, vv[j] << 16), acc[2 * j]);
    acc[2 * j + 1] = fmaf(pb, __builtin_bit_cast(float, vv[j] & 0xffff0000u), acc[2 * j + 1]);
  }
}

__global__ void __launch_bounds__(WAVES * 64, 1) attn_decode(const uint16_t* __restrict__ q, const uint16_t* __restrict__ k,
                                                             const uint16_t* __restrict__ v, uint16_t* __restrict__ out, int tensor_bytes) {
  __shared__ __attribute__((aligned(16))) float lds[LDS_FLOATS];
  const int tid = threadIdx.x, lane = tid & 63, wave = __builtin_amdgcn_readfirstlane(tid >> 6);
  const int r = lane >> 4, c = lane & 15;
  const bool b8 = c & 8, b4 = c & 4, b2 = c & 2, b1 = c & 1;
  constexpr float C2 = 0.088388347648318440f * 1.4426950408889634f;
  const int head0 = blockIdx.x * HPW;
  const int voff = lane * 16 + wave * 4096;
  u32x4 qv[HPW];
#pragma unroll
  for (int j = 0; j < HPW; ++j) qv[j] = *reinterpret_cast<const u32x4*>(q + (size_t)(head0 + j) * D + c * 8);
  const auto rk = __builtin_amdgcn_make_buffer_rsrc((void*)k, 0, tensor_bytes, 0x00020000);
  const auto rv = __builtin_amdgcn_make_buffer_rsrc((void*)v, 0, tensor_bytes, 0x00020000);
  int so = head0 * (SK * D * 2);
  u32x4 buf[NS];
#define LOADI(rs, base, i) __builtin_amdgcn_raw_buffer_load_b128(rs, voff + ((i) & 3) * 1024, (base) + ((i) >> 2) * 16384, 2)
#pragma unroll
  for (int u = 0; u < U; ++u) buf[u] = u < 16 ? LOADI(rk, so, u) : LOADI(rv, so, u - 16);
  __builtin_amdgcn_sched_barrier(0);
#pragma unroll
  for (int j = 0; j < HPW; ++j) {
    float m = -INFINITY, l = 0.f, acc[8];
#pragma unroll
    for (int i = 0; i < 8; ++i) acc[i] = 0.f;
    for (int ch = 0; ch < CHUNKS_PER_HEAD; ++ch) {
      const bool last = (j == HPW - 1) && (ch == CHUNKS_PER_HEAD - 1);
      const auto rkn = __builtin_amdgcn_make_buffer_rsrc((void*)k, 0, last ? 0 : tensor_bytes, 0x00020000);
      const auto rvn = __builtin_amdgcn_make_buffer_rsrc((void*)v, 0, last ? 0 : tensor_bytes, 0x00020000);
      float x[16];
#pragma unroll
      for (int i = 0; i < 16; ++i) {
        const u32x4 kk = buf[i];
        float d = dot2(kk[0], qv[j][0], 0.f);
        d = dot2(kk[1], qv[j][1], d); d = dot2(kk[2], qv[j][2], d); d = dot2(kk[3], qv[j][3], d);
        x[i] = d;
        asm volatile("" : "+v"(x[i]));
        const int n = i + U;
        buf[n % NS] = n < 16 ? LOADI(rk, so, n) : n < 32 ? LOADI(rv, so, n - 16) : LOADI(rkn, so + CHUNK_BYTES, n - 32);
        __builtin_amdgcn_sched_barrier(0);
        if (i & 1) { x[i - 1] = tr_step<XOR8>(x[i - 1], x[i], b8);
          if (i & 2) { x[i - 3] = tr_step<XOR7>(x[i - 3], x[i - 1], b4);
            if (i & 4) { x[i - 7] = tr_step<XOR2>(x[i - 7], x[i - 3], b2);
              if (i & 8) x[0] = tr_step<XOR1>(x[0], x[8], b1); } } }
      }
      const float s = x[0] * C2;
      float mx = fmaxf(s, dpp<XOR1>(s)); mx = fmaxf(mx, dpp<XOR2>(mx)); mx = fmaxf(mx, dpp<XOR7>(mx)); mx = fmaxf(mx, dpp<XOR8>(mx));
      const float mn = fmaxf(m, mx);
      const float alpha = __builtin_amdgcn_exp2f(m - mn);
      const float p = __builtin_amdgcn_exp2f(s - mn);
      m = mn;
      l = fmaf(l, alpha, p);
#pragma unroll
      for (int i = 0; i < 8; ++i) acc[i] *= alpha;
#define PVSTEP(i) { const u32x4 vv = buf[16 + i]; pv<i>(vv, p, acc); \
        asm volatile("" : "+v"(acc[0]), "+v"(acc[1]), "+v"(acc[2]), "+v"(acc[3]), "+v"(acc[4]), "+v"(acc[5]), "+v"(acc[6]), "+v"(acc[7])); \
        constexpr int n = 16 + i + U; buf[n % NS] = n < 32 ? LOADI(rv, so, n - 16) : n < 48 ? LOADI(rkn, so + CHUNK_BYTES, n - 32) : LOADI(rvn, so + CHUNK_BYTES, n - 48); __builtin_amdgcn_sched_barrier(0); }
      PVSTEP(0) PVSTEP(1) PVSTEP(2) PVSTEP(3) PVSTEP(4) PVSTEP(5) PVSTEP(6) PVSTEP(7)
      PVSTEP(8) PVSTEP(9) PVSTEP(10) PVSTEP(11) PVSTEP(12) PVSTEP(13) PVSTEP(14) PVSTEP(15)
      so += CHUNK_BYTES;
    }
    l += dpp<XOR1>(l); l += dpp<XOR2>(l); l += dpp<XOR7>(l); l += dpp<XOR8>(l);
    float* rec = lds + ((j * WAVES + wave) * 4 + r) * REC;
    *reinterpret_cast<f32x4*>(rec + 4 + 8 * c) = f32x4{acc[0], acc[1], acc[2], acc[3]};
    *reinterpret_cast<f32x4*>(rec + 8 + 8 * c) = f32x4{acc[4], acc[5], acc[6], acc[7]};
    if (c == 0) { rec[0] = m; rec[1] = l; }
  }
  __syncthreads();
  {
    const float* recs = lds + wave * 16 * REC;
    float M = -INFINITY;
#pragma unroll
    for (int w = 0; w < 16; ++w) M = fmaxf(M, recs[w * REC]);
    float L = 0.f, o0 = 0.f, o1 = 0.f;
#pragma unroll
    for (int w = 0; w < 16; ++w) {
      const float* rw = recs + w * REC;
      const float wt = __builtin_amdgcn_exp2f(rw[0] - M);
      L = fmaf(wt, rw[1], L);
      o0 = fmaf(wt, rw[4 + 2 * lane], o0);
      o1 = fmaf(wt, rw[5 + 2 * lane], o1);
    }
    const float inv = 1.f / L;
    __hip_bfloat162 o = __halves2bfloat162(__float2bfloat16(o0 * inv), __float2bfloat16(o1 * inv));
    reinterpret_cast<__hip_bfloat162*>(out + (size_t)(head0 + wave) * D)[lane] = o;
  }
}

extern "C" void kernel_launch(void* const* d_in, const int* in_sizes, int n_in, void* d_out, int out_size, void* d_ws,
                              size_t ws_size, hipStream_t stream) {
  const uint16_t* q = (const uint16_t*)d_in[0];
  const uint16_t* k = (const uint16_t*)d_in[1];
  const uint16_t* v = (const uint16_t*)d_in[2];
  const int heads = in_sizes[0] / D;
  attn_decode<<<heads / HPW, WAVES * 64, 0, stream>>>(q, k, v, (uint16_t*)d_out, in_sizes[1] * 2);
}
```

```cpp
#include <hip/hip_runtime.h>
#include <hip/hip_bf16.h>
#include <stdint.h>

typedef __attribute__((ext_vector_type(2))) __bf16 bf16x2_t;
typedef __attribute__((ext_vector_type(8))) __bf16 bf16x8_t;
typedef __attribute__((ext_vector_type(4))) unsigned u32x4;
typedef __attribute__((ext_vector_type(4))) float f32x4;

constexpr int D = 128;
constexpr int SK = 2048;
constexpr int NHEADS = 1024;
constexpr int HEAD_BYTES = SK * D * 2;
constexpr int TENSOR_BYTES = NHEADS * HEAD_BYTES;
constexpr int WAVES = 4;
constexpr int HPW = 4;
constexpr int NG = 8;
constexpr int U = 8;
constexpr int CAPN = 6;
constexpr int LDS_SC = 0, LDS_MX = LDS_SC + WAVES * HPW * NG * 64, LDS_Q = LDS_MX + WAVES * 64 * HPW, LDS_ACC = LDS_Q + HPW * D / 2,
              LDS_L = LDS_ACC + HPW * WAVES * 256, LDS_M = LDS_L + HPW * WAVES * 64;
constexpr int LDS_FLOATS = 21 * 1024;
static_assert(LDS_M + WAVES * HPW <= LDS_FLOATS && HPW == 4, "lds");
#define TPACE 13630
#define OFF0 145
#define SEGN 4
#define SEGT 800
#define HALF 13
#define PAUSE 15
#define XSKEW 5
#define JUMP 120
#define QUANTUM 240
#define BOOSTG 8
#define BOOSTL 50
#define EARLYG 6
#define EARLYL 60
#define STAUX 17
#define CODE_BYTES 6336

__device__ __forceinline__ float dot2(unsigned k, unsigned q, float acc) {
  return __builtin_amdgcn_fdot2_f32_bf16(__builtin_bit_cast(bf16x2_t, k), __builtin_bit_cast(bf16x2_t, q), acc, false);
}
template <int CTRL> __device__ __forceinline__ float dpp(float x) {
  return __builtin_bit_cast(float, __builtin_amdgcn_mov_dpp(__builtin_bit_cast(int, x), CTRL, 0xf, 0xf, true));
}
constexpr int XOR1 = 0xB1, XOR2 = 0x4E, XOR7 = 0x141, XOR8 = 0x128, BCAST = 0x150;
template <int CTRL> __device__ __forceinline__ float tr_step(float x0, float x1, bool hi) {
  const float keep = hi ? x1 : x0, send = hi ? x0 : x1;
  return keep + dpp<CTRL>(send);
}
__device__ __forceinline__ float xrow_sum(float x) {
  auto s = __builtin_amdgcn_permlane16_swap(__float_as_uint(x), __float_as_uint(x), false, false);
  x = __uint_as_float(s[0]) + __uint_as_float(s[1]);
  auto t = __builtin_amdgcn_permlane32_swap(__float_as_uint(x), __float_as_uint(x), false, false);
  return __uint_as_float(t[0]) + __uint_as_float(t[1]);
}
constexpr int lane_of(int t) { return ((t & 1) << 3) | ((t & 2) << 1) | ((t & 4) >> 1) | ((t & 8) >> 3); }

constexpr unsigned PMASK = (3u << 10) | (1u << 14) | (7u << 16);
__device__ __forceinline__ unsigned next_pair(unsigned po) { return ((po | ~PMASK) + 1) & PMASK; }

struct Pacer {
  unsigned t0;
  int nom, dnom;
  int jump, lag, late;
  __device__ __forceinline__ void wait() {
    unsigned due = t0 + (unsigned)(nom >> 10);
    asm volatile("s_branch 2f\n1:\n\ts_sleep 1\n2:\n\ts_memrealtime s[64:65]\n\ts_waitcnt lgkmcnt(0)\n\ts_sub_u32 s64, s64, %0\n\ts_cmp_lt_i32 s64, 0\n\ts_cbranch_scc1 1b\n\t"
                 "s_sub_u32 s65, s64, %2\n\ts_mov_b32 %2, s64\n\ts_cmp_le_i32 s65, %3\n\ts_cbranch_scc1 3f\n\ts_add_u32 %0, %0, %4\n\ts_add_u32 %1, %1, %4\n\ts_branch 2b\n3:"
                 : "+&s"(due), "+&s"(t0), "+&s"(lag) : "s"(jump), "s"(QUANTUM) : "s64", "s65", "scc", "memory");
    nom += dnom;
    __builtin_amdgcn_s_barrier();
  }
  __device__ __forceinline__ void before_first() {
    asm volatile("s_cmp_gt_i32 %0, %1\n\ts_cbranch_scc1 4f\n\ts_waitcnt vmcnt(%2)\n4:" :: "s"(lag), "s"(late), "n"(CAPN - 1) : "scc", "memory");
    wait();
  }
  __device__ __forceinline__ void before_second() {
    asm volatile("s_sleep 1\n\ts_nop 7\n\ts_waitcnt vmcnt(%3)\n\ts_cmp_gt_i32 %0, %1\n\ts_cbranch_scc1 4f\n\ts_waitcnt vmcnt(%2)\n4:"
                 :: "s"(lag), "s"(late), "n"(CAPN - 1), "n"(CAPN) : "scc", "memory");
  }
};

__global__ void __launch_bounds__(WAVES * 64, 1) attn_decode(const uint16_t* __restrict__ q, const uint16_t* __restrict__ k,
                                                             const uint16_t* __restrict__ v, uint16_t* __restrict__ out) {
  __shared__ __attribute__((aligned(16))) float lds[LDS_FLOATS];
  const int tid = threadIdx.x;
  const unsigned id = blockIdx.x;
  const unsigned blk = id ^ (((id ^ (id >> 6)) & 1) * 65);
  const unsigned base0 = blk * (HPW * HEAD_BYTES);
  unsigned po = (blk & 1) << 11;
  const unsigned voff = tid * 16 + (tid >> 6) * 3072;
  typedef const __attribute__((address_space(1))) char* gptr;
  typedef const __attribute__((address_space(1))) unsigned* gu32;
  typedef const __attribute__((address_space(1))) u32x4* gu128;
  const gptr pc0 = (gptr)__builtin_amdgcn_s_getpc();
  const unsigned ctouch = *(gu32)(pc0 + (unsigned)(tid * 16));
  const unsigned tstart = (unsigned)__builtin_amdgcn_s_memrealtime();
  __builtin_amdgcn_sched_barrier(0);
  u32x4 buf[16];
  const unsigned a0 = voff | base0 | po, a1 = a0 | 32768;
  buf[0] = __builtin_nontemporal_load((gu128)((gptr)k + a0)); __builtin_amdgcn_sched_barrier(0);
  buf[1] = __builtin_nontemporal_load((gu128)((gptr)k + a1)); __builtin_amdgcn_sched_barrier(0);
  buf[2] = __builtin_nontemporal_load((gu128)((gptr)k + a0 + 1024)); __builtin_amdgcn_sched_barrier(0);
  buf[3] = __builtin_nontemporal_load((gu128)((gptr)k + a1 + 1024)); __builtin_amdgcn_sched_barrier(0);
  const unsigned qw = *(gu32)((gptr)q + (unsigned)(tid * 4 | blk * (HPW * D * 2)));
  __builtin_amdgcn_sched_barrier(0);
  po = next_pair(next_pair(po));
  buf[4] = __builtin_nontemporal_load((gu128)((gptr)k + (voff | base0 | po))); __builtin_amdgcn_sched_barrier(0);
  buf[5] = __builtin_nontemporal_load((gu128)((gptr)k + (voff | base0 | po | 32768))); __builtin_amdgcn_sched_barrier(0);
  po = next_pair(po);
  static_assert(CAPN == 6 && U == 8, "prologue");
  const unsigned ctouch2 = *(gu32)(pc0 + (unsigned)(tid * 16) + (CODE_BYTES - 4096 - 64));
  __builtin_amdgcn_sched_barrier(0);
  const int lane = tid & 63, wave = __builtin_amdgcn_readfirstlane(tid >> 6);
  const int c = lane & 15;
  const int head0 = blk * HPW;
  const auto rk = __builtin_amdgcn_make_buffer_rsrc((void*)k, 0, TENSOR_BYTES, 0x00020000);
  const auto rv = __builtin_amdgcn_make_buffer_rsrc((void*)v, 0, TENSOR_BYTES, 0x00020000);
  const unsigned voff2 = voff | 32768;
#define LOAD_PAIR(paced, rs, hb, n) { const unsigned pa_ = (hb) | po; po = next_pair(po); \
    if (paced) pc.before_first(); \
    buf[(n) & 15] = __builtin_amdgcn_raw_buffer_load_b128(rs, voff, pa_, 2  ); __builtin_amdgcn_sched_barrier(0); \
    if (paced) pc.before_second(); \
    buf[((n) + 1) & 15] = __builtin_amdgcn_raw_buffer_load_b128(rs, voff2, pa_, 2); __builtin_amdgcn_sched_barrier(0); }
  const unsigned vtouch = __builtin_amdgcn_raw_buffer_load_b32(rv, c * 4, base0 + (HPW * HEAD_BYTES - 64), 2);
  const auto ro = __builtin_amdgcn_make_buffer_rsrc((void*)out, 0, NHEADS * D * 2, 0x00020000);
  const unsigned otouch = __builtin_amdgcn_raw_buffer_load_b32(ro, c * 4, head0 * (D * 2), 2);
  __builtin_amdgcn_sched_barrier(0);
  const unsigned xcc = __builtin_amdgcn_s_getreg((20 << 0) | (0 << 6) | (2 << 11)) & 7;
  const int tp = TPACE - (int)((0x689A4210u >> (4 * xcc)) & 15) * XSKEW;
  const bool b8 = c & 8, b4 = c & 4, b2 = c & 2, b1 = c & 1;
  float* sc = lds + LDS_SC + wave * (HPW * NG * 64) + lane;
  float* mxp = lds + LDS_MX + tid * HPW;
  *reinterpret_cast<f32x4*>(mxp) = f32x4{-INFINITY, -INFINITY, -INFINITY, -INFINITY};
  unsigned sel[4];
#pragma unroll
  for (int i = 0; i < 4; ++i) {
    const unsigned src = b8 ? 0x0302u : 0x0100u;
    sel[i] = ((c & 7) >> 1) == i ? (b1 ? (src << 16) | 0x0c0cu : 0x0c0c0000u | src) : 0x0c0c0c0cu;
    asm volatile("" : "+v"(sel[i]));
  }
  reinterpret_cast<unsigned*>(lds + LDS_Q)[tid] = qw;
  Pacer pc{tstart, (OFF0 + (int)(blk & 1) * HALF) << 10, 2 * (tp + SEGT), 1 << 20, 0, 1 << 20};
  LOAD_PAIR(true, rk, base0, 6)
  constexpr float C2 = 0.088388347648318440f * 1.4426950408889634f;
  constexpr int NGT = HPW * NG;

  u32x4 qv = *reinterpret_cast<const u32x4*>(lds + LDS_Q + c * 4);
#pragma unroll 1
  for (int gg = 0; gg < NGT; ++gg) {
    const int j = gg >> 3, jn = ((gg + 1) >> 3) & (HPW - 1);
    const unsigned cur = base0 + j * HEAD_BYTES, nxt = base0 + jn * HEAD_BYTES;
    const auto rn = __builtin_amdgcn_make_buffer_rsrc((void*)(gg + 1 == NGT ? v : k), 0, TENSOR_BYTES, 0x00020000);
    const u32x4 qn = *reinterpret_cast<const u32x4*>(lds + LDS_Q + jn * (D / 2) + c * 4);
    pc.jump = gg < 2 ? 1 << 20 : JUMP;
    pc.dnom = gg < SEGN ? 2 * (tp + SEGT) : 2 * tp;
    pc.late = gg < EARLYG ? EARLYL : 1 << 20;
    float x[16];
#pragma unroll
    for (int i = 0; i < 16; ++i) {
      const u32x4 kk = buf[i];
      float d = dot2(kk[0], qv[0], 0.f);
      d = dot2(kk[1], qv[1], d); d = dot2(kk[2], qv[2], d); d = dot2(kk[3], qv[3], d);
      x[i] = d;
      asm volatile("" : "+v"(x[i]));
      if (i & 1) {
        x[i - 1] = tr_step<XOR8>(x[i - 1], x[i], b8);
        if (i & 2) { x[i - 3] = tr_step<XOR7>(x[i - 3], x[i - 1], b4);
          if (i & 4) { x[i - 7] = tr_step<XOR2>(x[i - 7], x[i - 3], b2);
            if (i & 8) x[0] = tr_step<XOR1>(x[0], x[8], b1); } }
        __builtin_amdgcn_sched_barrier(0);
        if (i - 1 + U < 16) LOAD_PAIR(true, rk, cur, i - 1 + U) else LOAD_PAIR(true, rn, nxt, i - 1 + U)
      }
    }
    const float s = x[0] * C2;
    sc[gg * 64] = s;
    __hip_atomic_fetch_max(mxp + j, s, __ATOMIC_RELAXED, __HIP_MEMORY_SCOPE_WAVEFRONT);
    qv = qn;
  }
  asm volatile("" :: "v"(vtouch), "v"(ctouch), "v"(ctouch2), "v"(otouch));
  pc.nom += PAUSE << 10;
  float sn = sc[0];
  float mw[HPW];
  {
    const f32x4 m4 = *reinterpret_cast<const f32x4*>(mxp);
    float m0 = m4[0], m1 = m4[1], m2 = m4[2], m3 = m4[3];
#define MAX4(ctrl) "v_max_f32_dpp %0, %0, %0 " ctrl "\n\tv_max_f32_dpp %1, %1, %1 " ctrl "\n\tv_max_f32_dpp %2, %2, %2 " ctrl "\n\tv_max_f32_dpp %3, %3, %3 " ctrl "\n\t"
    asm volatile("s_nop 1\n\t" MAX4("quad_perm:[1,0,3,2] row_mask:0xf bank_mask:0xf") MAX4("quad_perm:[2,3,0,1] row_mask:0xf bank_mask:0xf")
                 MAX4("row_half_mirror row_mask:0xf bank_mask:0xf") MAX4("row_mirror row_mask:0xf bank_mask:0xf")
                 MAX4("row_bcast:15 row_mask:0xa bank_mask:0xf") MAX4("row_bcast:31 row_mask:0xc bank_mask:0xf") "s_nop 1"
                 : "+v"(m0), "+v"(m1), "+v"(m2), "+v"(m3));
#undef MAX4
    mw[0] = __builtin_bit_cast(float, __builtin_amdgcn_readlane(__builtin_bit_cast(int, m0), 63));
    mw[1] = __builtin_bit_cast(float, __builtin_amdgcn_readlane(__builtin_bit_cast(int, m1), 63));
    mw[2] = __builtin_bit_cast(float, __builtin_amdgcn_readlane(__builtin_bit_cast(int, m2), 63));
    mw[3] = __builtin_bit_cast(float, __builtin_amdgcn_readlane(__builtin_bit_cast(int, m3), 63));
    if (lane == 63) *reinterpret_cast<f32x4*>(lds + LDS_M + wave * HPW) = f32x4{m0, m1, m2, m3};
  }

  float l = 0.f;
  f32x4 acc = {0.f, 0.f, 0.f, 0.f};
  float wt[WAVES] = {0.f, 0.f, 0.f, 0.f};
#pragma unroll 1
  for (int gg = 0; gg < NGT; ++gg) {
    const int j = gg >> 3, jn = ((gg + 1) >> 3) & (HPW - 1);
    const bool last = (gg + 1 == NGT);
    const unsigned cur = base0 + j * HEAD_BYTES, nxt = base0 + jn * HEAD_BYTES;
    const auto rn = __builtin_amdgcn_make_buffer_rsrc((void*)v, 0, last ? 0 : TENSOR_BYTES, 0x00020000);
    if (gg == BOOSTG) pc.late = BOOSTL;
    const float p = __builtin_amdgcn_exp2f(sn - mw[0]);
    sn = sc[((gg + 1) & (NGT - 1)) * 64];
    l += p;
    lds[LDS_L + j * WAVES * 64 + tid] = l;
    const __bf16 ph = (__bf16)p;
    const __bf16 pl = (__bf16)(p - (float)ph);
    const int px = __builtin_bit_cast(int, bf16x2_t{ph, pl});
#pragma unroll
    for (int i = 0; i < 16; ++i) {
      unsigned y;
      switch (i) {
#define BC(t) case t: y = (unsigned)__builtin_amdgcn_mov_dpp(px, BCAST + lane_of(t), 0xf, 0xf, true); break;
        BC(0) BC(1) BC(2) BC(3) BC(4) BC(5) BC(6) BC(7) BC(8) BC(9) BC(10) BC(11) BC(12) BC(13) BC(14)
        default: y = (unsigned)__builtin_amdgcn_mov_dpp(px, BCAST + lane_of(15), 0xf, 0xf, true);
#undef BC
      }
      const u32x4 b = {__builtin_amdgcn_perm(y, y, sel[0]), __builtin_amdgcn_perm(y, y, sel[1]), __builtin_amdgcn_perm(y, y, sel[2]), __builtin_amdgcn_perm(y, y, sel[3])};
      acc = __builtin_amdgcn_mfma_f32_16x16x32_bf16(__builtin_bit_cast(bf16x8_t, buf[i]), __builtin_bit_cast(bf16x8_t, b), acc, 0, 0, 0);
      __builtin_amdgcn_sched_barrier(0);
      if (i & 1) {
        if (i - 1 + U < 16) LOAD_PAIR(true, rv, cur, i - 1 + U) else LOAD_PAIR(!last, rn, nxt, i - 1 + U)
      }
      if (i == 16 - U - 1 && last) {
        float M = -INFINITY, L = 0.f;
#pragma unroll
        for (int w = 0; w < WAVES; ++w) M = fmaxf(M, lds[LDS_M + w * HPW + wave]);
#pragma unroll
        for (int w = 0; w < WAVES; ++w) {
          wt[w] = __builtin_amdgcn_exp2f(lds[LDS_M + w * HPW + wave] - M);
          L = fmaf(wt[w], lds[LDS_L + (wave * WAVES + w) * 64 + lane], L);
        }
        L += dpp<XOR1>(L); L += dpp<XOR2>(L); L += dpp<XOR7>(L); L += dpp<XOR8>(L);
        const float inv = 1.f / xrow_sum(L);
#pragma unroll
        for (int w = 0; w < WAVES; ++w) wt[w] *= inv;
        __builtin_amdgcn_sched_barrier(0);
      }
    }
    if ((gg & (NG - 1)) == NG - 1) {
      *reinterpret_cast<f32x4*>(lds + LDS_ACC + (j * WAVES * 64 + tid) * 4) = acc;
      acc = f32x4{0.f, 0.f, 0.f, 0.f};
      l = 0.f;
      mw[0] = mw[1]; mw[1] = mw[2]; mw[2] = mw[3];
    }
  }
  __syncthreads();
  {
    const int chunk = lane >> 2, n = 2 * (lane & 3);
    const float* ap = lds + LDS_ACC + wave * WAVES * 256 + ((chunk >> 2) * 16 + n) * 4 + (chunk & 3);
    float o0 = 0.f, o1 = 0.f;
#pragma unroll
    for (int w = 0; w < WAVES; ++w) {
      o0 = fmaf(wt[w], ap[w * 256] + ap[w * 256 + 32], o0);
      o1 = fmaf(wt[w], ap[w * 256 + 4] + ap[w * 256 + 36], o1);
    }
    const unsigned o = __builtin_bit_cast(unsigned, bf16x2_t{(__bf16)o0, (__bf16)o1});
    __builtin_amdgcn_raw_buffer_store_b32(o, ro, lane * 4, (head0 + wave) * (D * 2), STAUX);
  }
}

extern "C" void kernel_launch(void* const* d_in, const int* in_sizes, int n_in, void* d_out, int out_size, void* d_ws,
                              size_t ws_size, hipStream_t stream) {
  const uint16_t* q = (const uint16_t*)d_in[0];
  const uint16_t* k = (const uint16_t*)d_in[1];
  const uint16_t* v = (const uint16_t*)d_in[2];
  attn_decode<<<NHEADS / HPW, WAVES * 64, 0, stream>>>(q, k, v, (uint16_t*)d_out);
}
```
